```python
import torch

def _softmax(x, axis=-1):
    return torch.softmax(x, dim=axis)
def _gelu(x):
    return torch.nn.functional.gelu(x, approximate="tanh")
def _silu(x):
    return torch.nn.functional.silu(x)
def _sigmoid(x):
    return torch.sigmoid(x)
def _layernorm(x, axis=-1, eps=1e-5):
    m = x.mean(dim=axis, keepdim=True)
    v = x.var(dim=axis, keepdim=True, unbiased=False)
    return (x - m) / torch.sqrt(v + eps)
def _scatter_add(tgt, idx, src, axis=0):
    return tgt.clone().index_add_(axis, idx, src)
def _segment_sum(src, seg, n):
    out = torch.zeros((n,) + src.shape[1:], dtype=src.dtype)
    return out.index_add_(0, seg, src)
def _pad(x, pw):
    flat = tuple(v for pair in reversed(pw) for v in pair)
    return torch.nn.functional.pad(x, flat)

kernel_name = 'standalone__attention_interact_autoint__recsys'

def setup_inputs():

    _w = (lambda a: (lambda t: t.to(torch.bfloat16) if t.is_floating_point() else t)(torch.as_tensor(a)))
    B, F = 4096, 100
    D = max(2, (16 // 2) * 2)
    H = 2
    Dh = D // H
    return {
        "e": _w((lambda *s: torch.randn(*s, dtype=torch.bfloat16) * 0.05)(B, F, D).to(float)),
        "wq": _w((lambda *s: torch.randn(*s, dtype=torch.bfloat16) * 6.4)(H, D, Dh).to(float)),
        "wk": _w((lambda *s: torch.randn(*s, dtype=torch.bfloat16) * 6.4)(H, D, Dh).to(float)),
        "wv": _w((lambda *s: torch.randn(*s, dtype=torch.bfloat16) * 0.05)(H, D, Dh).to(float)),
        "wres": _w((lambda *s: torch.randn(*s, dtype=torch.bfloat16) * 0.05)(D, D).to(float)),
    }

def reference(e, wq, wk, wv, wres):
    H, D, Dh = wq.shape[0], wq.shape[1], wq.shape[2]
    B, F = e.shape[0], e.shape[1]
    q = torch.einsum('bfd,hdk->bhfk', e, wq)
    k = torch.einsum('bfd,hdk->bhfk', e, wk)
    v = torch.einsum('bfd,hdk->bhfk', e, wv)
    scale = float(Dh) ** -0.5
    scores = torch.einsum('bhfk,bhgk->bhfg', q, k) * scale
    attn = _softmax(scores, axis=-1)
    attn = (lambda x, dt: x.to(dt))(attn, v.dtype)
    ctx = torch.einsum('bhfg,bhgk->bhfk', attn, v)
    ctx = (lambda x, *d: x.permute(*d))(ctx, 0, 2, 1, 3).reshape(B, F, H * Dh)
    res = torch.einsum('bfd,de->bfe', e, wres)
    out = ctx + res
    mask = (lambda x, dt: x.to(dt))(out > 0, out.dtype)
    return out * mask
```

```cpp
#include <hip/hip_runtime.h>
#include <hip/hip_bf16.h>

#define F_ 100
#define D_ 16

__device__ __forceinline__ float bf2f(unsigned short h) { return __uint_as_float(((unsigned)h) << 16); }
__device__ __forceinline__ float rbf(float x) { return __bfloat162float(__float2bfloat16(x)); }

__global__ __launch_bounds__(256) void naive_kernel(const unsigned short* __restrict__ e, const unsigned short* __restrict__ wq,
    const unsigned short* __restrict__ wk, const unsigned short* __restrict__ wv, const unsigned short* __restrict__ wres,
    __hip_bfloat16* __restrict__ out) {
  __shared__ float se[F_][D_], sq[F_][D_], sk[F_][D_], sv[F_][D_], sr[F_][D_];
  __shared__ float ss[200][101];
  int b = blockIdx.x, tid = threadIdx.x;
  for (int i = tid; i < F_ * D_; i += 256) se[i / D_][i % D_] = bf2f(e[(size_t)b * F_ * D_ + i]);
  __syncthreads();
  for (int i = tid; i < F_ * D_; i += 256) {
    int f = i / D_, c = i % D_, h = c / 8, dh = c % 8;
    float q = 0, k = 0, v = 0, r = 0;
    for (int d = 0; d < D_; ++d) {
      float x = se[f][d];
      q += x * bf2f(wq[(h * D_ + d) * 8 + dh]);
      k += x * bf2f(wk[(h * D_ + d) * 8 + dh]);
      v += x * bf2f(wv[(h * D_ + d) * 8 + dh]);
      r += x * bf2f(wres[d * D_ + c]);
    }
    sq[f][c] = rbf(q); sk[f][c] = rbf(k); sv[f][c] = rbf(v); sr[f][c] = rbf(r);
  }
  __syncthreads();
  const float scale = 0.35355339059327373f;
  for (int i = tid; i < F_ * 2; i += 256) {
    int f = i >> 1, h = i & 1;
    float* s = ss[i];
    float m = -1e30f;
    for (int g = 0; g < F_; ++g) {
      float acc = 0;
      for (int k = 0; k < 8; ++k) acc += sq[f][h * 8 + k] * sk[g][h * 8 + k];
      acc = rbf(rbf(acc) * scale);
      s[g] = acc; m = fmaxf(m, acc);
    }
    float l = 0;
    for (int g = 0; g < F_; ++g) { s[g] = __expf(s[g] - m); l += s[g]; }
    float inv = 1.0f / l;
    float o[8] = {0,0,0,0,0,0,0,0};
    for (int g = 0; g < F_; ++g) {
      float p = rbf(s[g] * inv);
      for (int k = 0; k < 8; ++k) o[k] += p * sv[g][h * 8 + k];
    }
    for (int k = 0; k < 8; ++k) {
      float val = rbf(rbf(o[k]) + sr[f][h * 8 + k]);
      val = val > 0.f ? val : 0.f;
      out[((size_t)b * F_ + f) * D_ + h * 8 + k] = __float2bfloat16(val);
    }
  }
}

extern "C" void kernel_launch(void* const* d_in, const int* in_sizes, int n_in,
                              void* d_out, int out_size, void* d_ws, size_t ws_size,
                              hipStream_t stream) {
  const unsigned short* e = (const unsigned short*)d_in[0];
  const unsigned short* wq = (const unsigned short*)d_in[1];
  const unsigned short* wk = (const unsigned short*)d_in[2];
  const unsigned short* wv = (const unsigned short*)d_in[3];
  const unsigned short* wres = (const unsigned short*)d_in[4];
  int B = in_sizes[0] / (F_ * D_);
  naive_kernel<<<B, 256, 0, stream>>>(e, wq, wk, wv, wres, (__hip_bfloat16*)d_out);
}
```

```cpp
#include <hip/hip_runtime.h>
#include <hip/hip_bf16.h>
#include <stdint.h>

typedef __attribute__((ext_vector_type(8))) short bf16x8;
typedef __attribute__((ext_vector_type(16))) float f32x16;
typedef __attribute__((ext_vector_type(4))) unsigned u32x4;
typedef __attribute__((ext_vector_type(2))) unsigned u32x2;
typedef __attribute__((ext_vector_type(2))) __bf16 bf16x2;
typedef __attribute__((ext_vector_type(2))) unsigned short u16x2;
typedef __attribute__((ext_vector_type(2))) short s16x2;

#define DEV __device__ __forceinline__
#define F_ 100
#define D_ 16
#define SB() __builtin_amdgcn_sched_barrier(0)
#define REPEAT 1

DEV unsigned pk_bf16(float a, float b) {
  bf16x2 v;
  v[0] = (__bf16)a;
  v[1] = (__bf16)b;
  return __builtin_bit_cast(unsigned, v);
}
DEV f32x16 mfma32(u32x4 a, u32x4 b, f32x16 c) {
  return __builtin_amdgcn_mfma_f32_32x32x16_bf16(__builtin_bit_cast(bf16x8, a), __builtin_bit_cast(bf16x8, b), c, 0, 0, 0);
}
DEV float bfbits2f(unsigned short h) { return __uint_as_float(((unsigned)h) << 16); }
DEV unsigned short f2bfbits(float x) {
  __bf16 v = (__bf16)x;
  return __builtin_bit_cast(unsigned short, v);
}
DEV unsigned pkmax_u16(unsigned a, unsigned b) {
  u16x2 x = __builtin_bit_cast(u16x2, a), y = __builtin_bit_cast(u16x2, b);
  return __builtin_bit_cast(unsigned, __builtin_elementwise_max(x, y));
}
DEV unsigned pk_relu_bf16(unsigned a) {
  s16x2 x = __builtin_bit_cast(s16x2, a);
  s16x2 z = {0, 0};
  return __builtin_bit_cast(unsigned, __builtin_elementwise_max(x, z));
}
#define EXP2(x) __builtin_amdgcn_exp2f(x)

template <int NB>
__global__ __launch_bounds__(256, 2) void autoint_kernel(const unsigned short* __restrict__ e,
                                                         const unsigned short* __restrict__ wq,
                                                         const unsigned short* __restrict__ wk,
                                                         const unsigned short* __restrict__ wv,
                                                         const unsigned short* __restrict__ wres,
                                                         unsigned short* __restrict__ out, int nbatch) {
  const int lane = threadIdx.x & 63;
  const int r32 = lane & 31;
  const int h = lane >> 5;
  const int hd = r32 & 1;
  const int wave = __builtin_amdgcn_readfirstlane(blockIdx.x * 4 + (threadIdx.x >> 6));

  const float CSC = 0.35355339059327373f * 1.4426950408889634f;

  u32x4 Aqk, Bv, Ares;
  {
    unsigned short tq[8], tv[8], tr[8];
#pragma unroll
    for (int j = 0; j < 8; ++j) {
      const int d = 8 * h + j;
      const int c = r32 & 15;
      const int idx = ((c >> 3) * D_ + d) * 8 + (c & 7);
      unsigned short aq = wq[idx], ak = wk[idx], av = wv[idx], ar = wres[d * D_ + c];
      unsigned short sq = f2bfbits(bfbits2f(aq) * CSC);
      tq[j] = (r32 < 16) ? sq : ak;
      tv[j] = (r32 < 16) ? av : (unsigned short)0;
      tr[j] = (r32 < 16) ? ar : (unsigned short)0;
    }
#pragma unroll
    for (int i = 0; i < 4; ++i) {
      Aqk[i] = (unsigned)tq[2 * i] | ((unsigned)tq[2 * i + 1] << 16);
      Bv[i] = (unsigned)tv[2 * i] | ((unsigned)tv[2 * i + 1] << 16);
      Ares[i] = (unsigned)tr[2 * i] | ((unsigned)tr[2 * i + 1] << 16);
    }
  }
  const unsigned m0 = hd ? 0u : 0xFFFFFFFFu;
  const unsigned m1 = hd ? 0xFFFFFFFFu : 0u;
  const unsigned sg0 = 0x80008000u & m0, sg1 = 0x80008000u & m1;
  const unsigned halfmask = h ? 0u : 0xFFFFFFFFu;
  const float onesC = (r32 == 16 || r32 == 20) ? 1.0f : 0.0f;
  const int eq_off = (r32 >> 1) * 32 + 16 * h;
  const int st_off = 16 * r32 + 8 * h;

  f32x16 zero16;
#pragma unroll
  for (int i = 0; i < 16; ++i) zero16[i] = 0.f;

  for (int bi = 0; bi < NB * REPEAT; ++bi) {
    const int b = wave * NB + (bi % NB);
    if (b >= nbatch) break;
    const __amdgpu_buffer_rsrc_t rs_e =
        __builtin_amdgcn_make_buffer_rsrc((void*)(e + (size_t)b * (F_ * D_)), (short)0, F_ * D_ * 2, 0x00020000);
    const __amdgpu_buffer_rsrc_t rs_o =
        __builtin_amdgcn_make_buffer_rsrc((void*)(out + (size_t)b * (F_ * D_)), (short)0, F_ * D_ * 2, 0x00020000);
#define LOAD_EQ(t) __builtin_amdgcn_raw_buffer_load_b128(rs_e, eq_off + 512 * (t), 0, 0)

    u32x4 Kf[4];
    u32x4 Vf[7];
    u32x4 Akm;
    u32x4 eq_cur = LOAD_EQ(0);
    u32x4 eq_n1 = LOAD_EQ(1);
    {
      float mxk[8];
#pragma unroll
      for (int j = 0; j < 8; ++j) mxk[j] = 0.f;
      u32x4 ea[4];
#pragma unroll
      for (int tt = 0; tt < 4; ++tt)
        ea[tt] = __builtin_amdgcn_raw_buffer_load_b128(rs_e, (32 * tt + r32) * 32 + 16 * h, 0, 0);
      f32x16 oc;
#pragma unroll
      for (int i = 0; i < 16; ++i) oc[i] = onesC;
#pragma unroll
      for (int tt = 0; tt < 4; ++tt) {
        f32x16 dk = mfma32(Aqk, ea[tt], zero16);
        f32x16 dv = mfma32(ea[tt], Bv, oc);
#pragma unroll
        for (int i = 0; i < 4; ++i) Kf[tt][i] = pk_bf16(dk[8 + 2 * i], dk[9 + 2 * i]);
#pragma unroll
        for (int j = 0; j < 8; ++j) mxk[j] = fmaxf(mxk[j], fabsf(dk[8 + j]));
#pragma unroll
        for (int i = 0; i < 4; ++i) Vf[2 * tt][i] = pk_bf16(dv[2 * i], dv[2 * i + 1]);
        if (tt < 3) {
#pragma unroll
          for (int i = 0; i < 4; ++i) Vf[2 * tt + 1][i] = pk_bf16(dv[8 + 2 * i], dv[9 + 2 * i]);
        }
      }
#pragma unroll
      for (int i = 0; i < 4; ++i) Akm[i] = pk_bf16(mxk[2 * i], mxk[2 * i + 1]);
#pragma unroll
      for (int i = 0; i < 4; ++i) {
        Akm[i] = pkmax_u16(Akm[i], (unsigned)__builtin_amdgcn_ds_swizzle((int)Akm[i], 0x041F));
        Akm[i] = pkmax_u16(Akm[i], (unsigned)__builtin_amdgcn_ds_swizzle((int)Akm[i], 0x081F));
        Akm[i] = pkmax_u16(Akm[i], (unsigned)__builtin_amdgcn_ds_swizzle((int)Akm[i], 0x101F));
        Akm[i] = pkmax_u16(Akm[i], (unsigned)__builtin_amdgcn_ds_swizzle((int)Akm[i], 0x201F));
        Akm[i] = pkmax_u16(Akm[i], (unsigned)__builtin_amdgcn_ds_swizzle((int)Akm[i], 0x401F));
      }
    }

    u32x4 Qs;
    f32x16 Cb, S0;
    {
      f32x16 dq = mfma32(Aqk, eq_cur, zero16);
      unsigned q0 = pk_bf16(dq[0], dq[1]), q1 = pk_bf16(dq[2], dq[3]);
      unsigned q2 = pk_bf16(dq[4], dq[5]), q3 = pk_bf16(dq[6], dq[7]);
      Qs[0] = q0 & m0; Qs[1] = q1 & m0; Qs[2] = q2 & m1; Qs[3] = q3 & m1;
      u32x4 Qa;
      Qa[0] = Qs[0] | sg0; Qa[1] = Qs[1] | sg0; Qa[2] = Qs[2] | sg1; Qa[3] = Qs[3] | sg1;
      Cb = mfma32(Akm, Qa, zero16);
      S0 = mfma32(Kf[0], Qs, Cb);
    }
    f32x16 O = zero16;
    O[8] = 1.0f;
    u32x4 eq_prev = eq_cur;
    unsigned failmask = 0;

#define EP1()                                                   \
    {                                                           \
      const float l = O[8];                                     \
      const float rl = __builtin_amdgcn_rcpf(l);                \
      if (__builtin_amdgcn_ballot_w64(!(l >= 1e-10f)) != 0ull)  \
        failmask |= (1u << (tp < 0 ? 0 : tp));                                 \
      _Pragma("unroll") for (int i = 0; i < 8; ++i) O[i] *= rl; \
      O = mfma32(Ares, eq_prev, O);                             \
    }
#define EP2()                                                                   \
    {                                                                           \
      float v0 = hd ? O[4] : O[0], v1 = hd ? O[5] : O[1];                       \
      float v2 = hd ? O[6] : O[2], v3 = hd ? O[7] : O[3];                       \
      u32x2 ov;                                                                 \
      ov[0] = pk_relu_bf16(pk_bf16(v0, v1));                                    \
      ov[1] = pk_relu_bf16(pk_bf16(v2, v3));                                    \
      __builtin_amdgcn_raw_buffer_store_b64(ov, rs_o, st_off + 512 * tp, 0, 0); \
    }

#pragma unroll
    for (int t = 0; t < 7; ++t) {
      const int tp = t - 1;
      u32x4 eq_n2 = LOAD_EQ(t + 2);
      float p[16];
      u32x4 pa, pb;
      f32x16 S1, S2, S3;
#pragma unroll
      for (int i = 0; i < 4; ++i) p[i] = EXP2(S0[i]);
      S1 = mfma32(Kf[1], Qs, Cb);
#pragma unroll
      for (int i = 4; i < 8; ++i) p[i] = EXP2(S0[i]);
      EP1();
#pragma unroll
      for (int i = 0; i < 4; ++i) pa[i] = pk_bf16(p[2 * i], p[2 * i + 1]);
#pragma unroll
      for (int i = 8; i < 16; ++i) p[i] = EXP2(S0[i]);
      EP2();
      O = mfma32(Vf[0], pa, zero16);
#pragma unroll
      for (int i = 0; i < 4; ++i) pb[i] = pk_bf16(p[8 + 2 * i], p[9 + 2 * i]);
#pragma unroll
      for (int i = 0; i < 4; ++i) p[i] = EXP2(S1[i]);
      O = mfma32(Vf[1], pb, O);
#pragma unroll
      for (int i = 4; i < 8; ++i) p[i] = EXP2(S1[i]);
      S2 = mfma32(Kf[2], Qs, Cb);
#pragma unroll
      for (int i = 0; i < 4; ++i) pa[i] = pk_bf16(p[2 * i], p[2 * i + 1]);
#pragma unroll
      for (int i = 8; i < 12; ++i) p[i] = EXP2(S1[i]);
      O = mfma32(Vf[2], pa, O);
#pragma unroll
      for (int i = 12; i < 16; ++i) p[i] = EXP2(S1[i]);
      f32x16 dq = mfma32(Aqk, eq_n1, zero16);
#pragma unroll
      for (int i = 0; i < 4; ++i) pb[i] = pk_bf16(p[8 + 2 * i], p[9 + 2 * i]);
#pragma unroll
      for (int i = 0; i < 4; ++i) p[i] = EXP2(S2[i]);
      O = mfma32(Vf[3], pb, O);
#pragma unroll
      for (int i = 4; i < 8; ++i) p[i] = EXP2(S2[i]);
      S3 = mfma32(Kf[3], Qs, Cb);
      u32x4 Qsn, Qa;
      {
#pragma unroll
        for (int i = 0; i < 4; ++i) pa[i] = pk_bf16(p[2 * i], p[2 * i + 1]);
#pragma unroll
        for (int i = 8; i < 10; ++i) p[i] = EXP2(S2[i]);
        unsigned q0 = pk_bf16(dq[0], dq[1]), q1 = pk_bf16(dq[2], dq[3]);
        unsigned q2 = pk_bf16(dq[4], dq[5]), q3 = pk_bf16(dq[6], dq[7]);
        Qsn[0] = q0 & m0; Qsn[1] = q1 & m0; Qsn[2] = q2 & m1; Qsn[3] = q3 & m1;
        Qa[0] = Qsn[0] | sg0; Qa[1] = Qsn[1] | sg0; Qa[2] = Qsn[2] | sg1; Qa[3] = Qsn[3] | sg1;
      }
      O = mfma32(Vf[4], pa, O);
#pragma unroll
      for (int i = 10; i < 14; ++i) p[i] = EXP2(S2[i]);
      f32x16 Cbn = mfma32(Akm, Qa, zero16);
#pragma unroll
      for (int i = 14; i < 16; ++i) p[i] = EXP2(S2[i]);
#pragma unroll
      for (int i = 0; i < 4; ++i) pb[i] = pk_bf16(p[8 + 2 * i], p[9 + 2 * i]);
#pragma unroll
      for (int i = 0; i < 2; ++i) p[i] = EXP2(S3[i]);
      O = mfma32(Vf[5], pb, O);
#pragma unroll
      for (int i = 2; i < 4; ++i) p[i] = EXP2(S3[i]);
      pa[0] = pk_bf16(p[0], p[1]) & halfmask;
      pa[1] = pk_bf16(p[2], p[3]) & halfmask;
      pa[2] = 0u; pa[3] = 0u;
      S0 = mfma32(Kf[0], Qsn, Cbn);
      O = mfma32(Vf[6], pa, O);
#pragma unroll
      for (int g = 0; g < 13; ++g) {
        __builtin_amdgcn_sched_group_barrier(0x400, 4, 0);
        __builtin_amdgcn_sched_group_barrier(0x8, 1, 0);
      }
      __builtin_amdgcn_sched_group_barrier(0x8, 1, 0);
      Qs = Qsn;
      Cb = Cbn;
      eq_prev = eq_cur;
      eq_cur = eq_n1;
      eq_n1 = eq_n2;
    }
    {
      const int tp = 6;
      EP1();
      EP2();
    }
#undef EP1
#undef EP2

    if (failmask) {
      for (int t = 0; t < 7; ++t) {
        if (!((failmask >> t) & 1u)) continue;
        u32x4 eq = LOAD_EQ(t);
        f32x16 dq = mfma32(Aqk, eq, zero16);
        u32x4 Qx;
        Qx[0] = pk_bf16(dq[0], dq[1]) & m0; Qx[1] = pk_bf16(dq[2], dq[3]) & m0;
        Qx[2] = pk_bf16(dq[4], dq[5]) & m1; Qx[3] = pk_bf16(dq[6], dq[7]) & m1;
        float mx = -INFINITY;
#pragma unroll
        for (int kt = 0; kt < 4; ++kt) {
          f32x16 S = mfma32(Kf[kt], Qx, zero16);
          if (kt < 3) {
#pragma unroll
            for (int i = 0; i < 16; ++i) mx = fmaxf(mx, S[i]);
          } else {
#pragma unroll
            for (int i = 0; i < 4; ++i) mx = fmaxf(mx, h ? -INFINITY : S[i]);
          }
        }
        mx = fmaxf(mx, __shfl_xor(mx, 32));
        f32x16 Cx;
#pragma unroll
        for (int i = 0; i < 16; ++i) Cx[i] = -mx;
        f32x16 Ox = zero16;
#pragma unroll
        for (int kt = 0; kt < 4; ++kt) {
          f32x16 S = mfma32(Kf[kt], Qx, Cx);
          if (kt < 3) {
            u32x4 p0, p1;
#pragma unroll
            for (int i = 0; i < 4; ++i) {
              p0[i] = pk_bf16(EXP2(S[2 * i]), EXP2(S[2 * i + 1]));
              p1[i] = pk_bf16(EXP2(S[8 + 2 * i]), EXP2(S[9 + 2 * i]));
            }
            Ox = mfma32(Vf[2 * kt], p0, Ox);
            Ox = mfma32(Vf[2 * kt + 1], p1, Ox);
          } else {
            u32x4 p0;
            p0[0] = pk_bf16(EXP2(S[0]), EXP2(S[1])) & halfmask;
            p0[1] = pk_bf16(EXP2(S[2]), EXP2(S[3])) & halfmask;
            p0[2] = 0u; p0[3] = 0u;
            Ox = mfma32(Vf[6], p0, Ox);
          }
        }
        const float rl = __builtin_amdgcn_rcpf(Ox[8]);
#pragma unroll
        for (int i = 0; i < 8; ++i) Ox[i] *= rl;
        Ox = mfma32(Ares, eq, Ox);
        float v0 = hd ? Ox[4] : Ox[0], v1 = hd ? Ox[5] : Ox[1];
        float v2 = hd ? Ox[6] : Ox[2], v3 = hd ? Ox[7] : Ox[3];
        u32x2 ov;
        ov[0] = pk_relu_bf16(pk_bf16(v0, v1));
        ov[1] = pk_relu_bf16(pk_bf16(v2, v3));
        __builtin_amdgcn_raw_buffer_store_b64(ov, rs_o, st_off + 512 * t, 0, 0);
      }
    }
#undef LOAD_EQ
  }
}

extern "C" void kernel_launch(void* const* d_in, const int* in_sizes, int n_in, void* d_out, int out_size, void* d_ws,
                              size_t ws_size, hipStream_t stream) {
  const unsigned short* e = (const unsigned short*)d_in[0];
  const unsigned short* wq = (const unsigned short*)d_in[1];
  const unsigned short* wk = (const unsigned short*)d_in[2];
  const unsigned short* wv = (const unsigned short*)d_in[3];
  const unsigned short* wres = (const unsigned short*)d_in[4];
  const int nbatch = in_sizes[0] / (F_ * D_);
  constexpr int NB = 2;
  const int nwaves = (nbatch + NB - 1) / NB;
  const int nblocks = (nwaves + 3) / 4;
  autoint_kernel<NB><<<nblocks, 256, 0, stream>>>(e, wq, wk, wv, wres, (unsigned short*)d_out, nbatch);
}
```

```cpp
#include <hip/hip_runtime.h>
#include <hip/hip_bf16.h>
#include <stdint.h>


typedef __attribute__((ext_vector_type(8))) short bf16x8;
typedef __attribute__((ext_vector_type(16))) float f32x16;
typedef __attribute__((ext_vector_type(4))) unsigned u32x4;
typedef __attribute__((ext_vector_type(2))) unsigned u32x2;
typedef __attribute__((ext_vector_type(2))) __bf16 bf16x2;
typedef __attribute__((ext_vector_type(2))) unsigned short u16x2;

#define DEV __device__ __forceinline__
#define F_ 100
#define D_ 16

DEV unsigned pk_bf16(float a, float b) {
  bf16x2 v;
  v[0] = (__bf16)a;
  v[1] = (__bf16)b;
  return __builtin_bit_cast(unsigned, v);
}
DEV f32x16 mfma32(u32x4 a, u32x4 b, f32x16 c) {
  return __builtin_amdgcn_mfma_f32_32x32x16_bf16(__builtin_bit_cast(bf16x8, a), __builtin_bit_cast(bf16x8, b), c, 0, 0, 0);
}
DEV float bfbits2f(unsigned short h) { return __uint_as_float(((unsigned)h) << 16); }
DEV unsigned short f2bfbits(float x) {
  __bf16 v = (__bf16)x;
  return __builtin_bit_cast(unsigned short, v);
}
DEV unsigned pkmax_u16(unsigned a, unsigned b) {
  u16x2 x = __builtin_bit_cast(u16x2, a), y = __builtin_bit_cast(u16x2, b);
  return __builtin_bit_cast(unsigned, __builtin_elementwise_max(x, y));
}

template <int NB>
__global__ __launch_bounds__(256, 4) void autoint_kernel(const unsigned short* __restrict__ e,
                                                         const unsigned short* __restrict__ wq,
                                                         const unsigned short* __restrict__ wk,
                                                         const unsigned short* __restrict__ wv,
                                                         const unsigned short* __restrict__ wres,
                                                         unsigned short* __restrict__ out, int nbatch) {
  const int lane = threadIdx.x & 63;
  const int r32 = lane & 31;
  const int h = lane >> 5;
  const int hd = r32 & 1;
  const int wave = __builtin_amdgcn_readfirstlane(blockIdx.x * 4 + (threadIdx.x >> 6));

  const float CSC = 0.35355339059327373f * 1.4426950408889634f;

  u32x4 Aqk, Bv, Ares;
  {
    unsigned short tq[8], tv[8], tr[8];
#pragma unroll
    for (int j = 0; j < 8; ++j) {
      const int d = 8 * h + j;
      const int c = r32 & 15;
      const int idx = ((c >> 3) * D_ + d) * 8 + (c & 7);
      unsigned short aq = wq[idx], ak = wk[idx], av = wv[idx], ar = wres[d * D_ + c];
      unsigned short sq = f2bfbits(bfbits2f(aq) * CSC);
      tq[j] = (r32 < 16) ? sq : ak;
      tv[j] = (r32 < 16) ? av : (unsigned short)0;
      tr[j] = (r32 < 16) ? ar : (unsigned short)0;
    }
#pragma unroll
    for (int i = 0; i < 4; ++i) {
      Aqk[i] = (unsigned)tq[2 * i] | ((unsigned)tq[2 * i + 1] << 16);
      Bv[i] = (unsigned)tv[2 * i] | ((unsigned)tv[2 * i + 1] << 16);
      Ares[i] = (unsigned)tr[2 * i] | ((unsigned)tr[2 * i + 1] << 16);
    }
  }
  const unsigned m0 = hd ? 0u : 0xFFFFFFFFu;
  const unsigned m1 = hd ? 0xFFFFFFFFu : 0u;
  const unsigned sg0 = 0x80008000u & m0, sg1 = 0x80008000u & m1;
  const unsigned halfmask = h ? 0u : 0xFFFFFFFFu;
  const float onesC = (r32 == 16 || r32 == 20) ? 1.0f : 0.0f;

  f32x16 zero16;
#pragma unroll
  for (int i = 0; i < 16; ++i) zero16[i] = 0.f;

  for (int bi = 0; bi < NB; ++bi) {
    const int b = wave * NB + bi;
    if (b >= nbatch) break;
    const __amdgpu_buffer_rsrc_t rs_e =
        __builtin_amdgcn_make_buffer_rsrc((void*)(e + (size_t)b * (F_ * D_)), (short)0, F_ * D_ * 2, 0x00020000);
    const __amdgpu_buffer_rsrc_t rs_o =
        __builtin_amdgcn_make_buffer_rsrc((void*)(out + (size_t)b * (F_ * D_)), (short)0, F_ * D_ * 2, 0x00020000);

    u32x4 Kf[4];
    u32x4 Vf[7];
    float mxk[8];
#pragma unroll
    for (int j = 0; j < 8; ++j) mxk[j] = 0.f;
    {
      u32x4 ea[4];
#pragma unroll
      for (int tt = 0; tt < 4; ++tt)
        ea[tt] = __builtin_amdgcn_raw_buffer_load_b128(rs_e, (32 * tt + r32) * 32 + 16 * h, 0, 0);
      f32x16 oc;
#pragma unroll
      for (int i = 0; i < 16; ++i) oc[i] = onesC;
#pragma unroll
      for (int tt = 0; tt < 4; ++tt) {
        f32x16 dk = mfma32(Aqk, ea[tt], zero16);
#pragma unroll
        for (int i = 0; i < 4; ++i) Kf[tt][i] = pk_bf16(dk[8 + 2 * i], dk[9 + 2 * i]);
#pragma unroll
        for (int j = 0; j < 8; ++j) mxk[j] = fmaxf(mxk[j], fabsf(dk[8 + j]));
        f32x16 dv = mfma32(ea[tt], Bv, oc);
#pragma unroll
        for (int i = 0; i < 4; ++i) Vf[2 * tt][i] = pk_bf16(dv[2 * i], dv[2 * i + 1]);
        if (tt < 3) {
#pragma unroll
          for (int i = 0; i < 4; ++i) Vf[2 * tt + 1][i] = pk_bf16(dv[8 + 2 * i], dv[9 + 2 * i]);
        }
      }
    }
    u32x4 Akm;
#pragma unroll
    for (int i = 0; i < 4; ++i) Akm[i] = pk_bf16(mxk[2 * i], mxk[2 * i + 1]);
#pragma unroll
    for (int i = 0; i < 4; ++i) {
      Akm[i] = pkmax_u16(Akm[i], (unsigned)__builtin_amdgcn_ds_swizzle((int)Akm[i], 0x041F));
      Akm[i] = pkmax_u16(Akm[i], (unsigned)__builtin_amdgcn_ds_swizzle((int)Akm[i], 0x081F));
      Akm[i] = pkmax_u16(Akm[i], (unsigned)__builtin_amdgcn_ds_swizzle((int)Akm[i], 0x101F));
      Akm[i] = pkmax_u16(Akm[i], (unsigned)__builtin_amdgcn_ds_swizzle((int)Akm[i], 0x201F));
      Akm[i] = pkmax_u16(Akm[i], (unsigned)__builtin_amdgcn_ds_swizzle((int)Akm[i], 0x401F));
    }

    for (int t = 0; t < 7; ++t) {
      u32x4 eq = __builtin_amdgcn_raw_buffer_load_b128(rs_e, (16 * t + (r32 >> 1)) * 32 + 16 * h, 0, 0);
      u32x4 Qs, Qa;
      {
        f32x16 dq = mfma32(Aqk, eq, zero16);
        unsigned q0 = pk_bf16(dq[0], dq[1]), q1 = pk_bf16(dq[2], dq[3]);
        unsigned q2 = pk_bf16(dq[4], dq[5]), q3 = pk_bf16(dq[6], dq[7]);
        Qs[0] = q0 & m0; Qs[1] = q1 & m0; Qs[2] = q2 & m1; Qs[3] = q3 & m1;
        Qa[0] = Qs[0] | sg0; Qa[1] = Qs[1] | sg0; Qa[2] = Qs[2] | sg1; Qa[3] = Qs[3] | sg1;
      }
      f32x16 Cb = mfma32(Akm, Qa, zero16);

      f32x16 O;
      for (int attempt = 0; attempt < 2; ++attempt) {
        O = zero16;
#pragma unroll
        for (int kt = 0; kt < 4; ++kt) {
          f32x16 S = mfma32(Kf[kt], Qs, Cb);
          if (kt < 3) {
            u32x4 p0, p1;
#pragma unroll
            for (int i = 0; i < 4; ++i) {
              p0[i] = pk_bf16(__builtin_amdgcn_exp2f(S[2 * i]), __builtin_amdgcn_exp2f(S[2 * i + 1]));
              p1[i] = pk_bf16(__builtin_amdgcn_exp2f(S[8 + 2 * i]), __builtin_amdgcn_exp2f(S[9 + 2 * i]));
            }
            O = mfma32(Vf[2 * kt], p0, O);
            O = mfma32(Vf[2 * kt + 1], p1, O);
          } else {
            u32x4 p0;
            p0[0] = pk_bf16(__builtin_amdgcn_exp2f(S[0]), __builtin_amdgcn_exp2f(S[1])) & halfmask;
            p0[1] = pk_bf16(__builtin_amdgcn_exp2f(S[2]), __builtin_amdgcn_exp2f(S[3])) & halfmask;
            p0[2] = 0u; p0[3] = 0u;
            O = mfma32(Vf[6], p0, O);
          }
        }
        if (attempt == 1) break;
        const bool bad = !(O[8] >= 1e-10f);
        if (__builtin_amdgcn_ballot_w64(bad) == 0ull) break;
        float mx = -INFINITY;
#pragma unroll
        for (int kt = 0; kt < 4; ++kt) {
          f32x16 S = mfma32(Kf[kt], Qs, zero16);
          if (kt < 3) {
#pragma unroll
            for (int i = 0; i < 16; ++i) mx = fmaxf(mx, S[i]);
          } else {
#pragma unroll
            for (int i = 0; i < 4; ++i) mx = fmaxf(mx, h ? -INFINITY : S[i]);
          }
        }
        mx = fmaxf(mx, __shfl_xor(mx, 32));
#pragma unroll
        for (int i = 0; i < 16; ++i) Cb[i] = -mx;
      }
      const float rl = __builtin_amdgcn_rcpf(O[8]);
#pragma unroll
      for (int i = 0; i < 8; ++i) O[i] *= rl;
      f32x16 Dout = mfma32(Ares, eq, O);
      float v0 = hd ? Dout[4] : Dout[0];
      float v1 = hd ? Dout[5] : Dout[1];
      float v2 = hd ? Dout[6] : Dout[2];
      float v3 = hd ? Dout[7] : Dout[3];
      u32x2 ov;
      ov[0] = pk_bf16(fmaxf(v0, 0.f), fmaxf(v1, 0.f));
      ov[1] = pk_bf16(fmaxf(v2, 0.f), fmaxf(v3, 0.f));
      __builtin_amdgcn_raw_buffer_store_b64(ov, rs_o, 512 * t + 16 * r32 + 8 * h, 0, 0);
    }
  }
}

extern "C" void kernel_launch(void* const* d_in, const int* in_sizes, int n_in, void* d_out, int out_size, void* d_ws,
                              size_t ws_size, hipStream_t stream) {
  const unsigned short* e = (const unsigned short*)d_in[0];
  const unsigned short* wq = (const unsigned short*)d_in[1];
  const unsigned short* wk = (const unsigned short*)d_in[2];
  const unsigned short* wv = (const unsigned short*)d_in[3];
  const unsigned short* wres = (const unsigned short*)d_in[4];
  const int nbatch = in_sizes[0] / (F_ * D_);
  constexpr int NB = 1;
  const int nwaves = (nbatch + NB - 1) / NB;
  const int nblocks = (nwaves + 3) / 4;
  autoint_kernel<NB><<<nblocks, 256, 0, stream>>>(e, wq, wk, wv, wres, (unsigned short*)d_out, nbatch);
}
```

```cpp
#include <hip/hip_runtime.h>
#include <hip/hip_bf16.h>
#include <stdint.h>

typedef __attribute__((ext_vector_type(8))) short bf16x8;
typedef __attribute__((ext_vector_type(16))) float f32x16;
typedef __attribute__((ext_vector_type(4))) unsigned u32x4;
typedef __attribute__((ext_vector_type(2))) unsigned u32x2;
typedef __attribute__((ext_vector_type(2))) __bf16 bf16x2;
typedef __attribute__((ext_vector_type(2))) unsigned short u16x2;
typedef __attribute__((ext_vector_type(2))) short s16x2;

#define DEV __device__ __forceinline__
#define F_ 100
#define D_ 16
#define SB() __builtin_amdgcn_sched_barrier(0)
#define REPEAT 1

DEV unsigned pk_bf16(float a, float b) {
  bf16x2 v;
  v[0] = (__bf16)a;
  v[1] = (__bf16)b;
  return __builtin_bit_cast(unsigned, v);
}
DEV f32x16 mfma32(u32x4 a, u32x4 b, f32x16 c) {
  return __builtin_amdgcn_mfma_f32_32x32x16_bf16(__builtin_bit_cast(bf16x8, a), __builtin_bit_cast(bf16x8, b), c, 0, 0, 0);
}
DEV float bfbits2f(unsigned short h) { return __uint_as_float(((unsigned)h) << 16); }
DEV unsigned short f2bfbits(float x) {
  __bf16 v = (__bf16)x;
  return __builtin_bit_cast(unsigned short, v);
}
DEV unsigned pkmax_u16(unsigned a, unsigned b) {
  u16x2 x = __builtin_bit_cast(u16x2, a), y = __builtin_bit_cast(u16x2, b);
  return __builtin_bit_cast(unsigned, __builtin_elementwise_max(x, y));
}
DEV unsigned pk_relu_bf16(unsigned a) {
  s16x2 x = __builtin_bit_cast(s16x2, a);
  s16x2 z = {0, 0};
  return __builtin_bit_cast(unsigned, __builtin_elementwise_max(x, z));
}
#define EXP2(x) __builtin_amdgcn_exp2f(x)

template <int NB>
__global__ __launch_bounds__(256, 2) void autoint_kernel(const unsigned short* __restrict__ e,
                                                         const unsigned short* __restrict__ wq,
                                                         const unsigned short* __restrict__ wk,
                                                         const unsigned short* __restrict__ wv,
                                                         const unsigned short* __restrict__ wres,
                                                         unsigned short* __restrict__ out, int nbatch) {
  const int lane = threadIdx.x & 63;
  const int r32 = lane & 31;
  const int h = lane >> 5;
  const int hd = r32 & 1;
  const int wave = __builtin_amdgcn_readfirstlane(blockIdx.x * 4 + (threadIdx.x >> 6));

  const float CSC = 0.35355339059327373f * 1.4426950408889634f;

  unsigned touch0;
  {
    const int b0 = wave * NB;
    const __amdgpu_buffer_rsrc_t rs0 =
        __builtin_amdgcn_make_buffer_rsrc((void*)(e + (size_t)b0 * (F_ * D_)), (short)0, (b0 < nbatch) ? F_ * D_ * 2 : 0, 0x00020000);
    touch0 = __builtin_amdgcn_raw_buffer_load_b32(rs0, lane * 64, 0, 0);
  }
  SB();
  u32x4 Aqk, Bv, Ares;
  {
    unsigned short tq[8], tv[8], tr[8];
#pragma unroll
    for (int j = 0; j < 8; ++j) {
      const int d = 8 * h + j;
      const int c = r32 & 15;
      const int idx = ((c >> 3) * D_ + d) * 8 + (c & 7);
      unsigned short aq = wq[idx], ak = wk[idx], av = wv[idx], ar = wres[d * D_ + c];
      unsigned short sq = f2bfbits(bfbits2f(aq) * CSC);
      tq[j] = (r32 < 16) ? sq : ak;
      tv[j] = (r32 < 16) ? av : (unsigned short)0;
      tr[j] = (r32 < 16) ? ar : (unsigned short)0;
    }
#pragma unroll
    for (int i = 0; i < 4; ++i) {
      Aqk[i] = (unsigned)tq[2 * i] | ((unsigned)tq[2 * i + 1] << 16);
      Bv[i] = (unsigned)tv[2 * i] | ((unsigned)tv[2 * i + 1] << 16);
      Ares[i] = (unsigned)tr[2 * i] | ((unsigned)tr[2 * i + 1] << 16);
    }
  }
  const unsigned m0 = hd ? 0u : 0xFFFFFFFFu;
  const unsigned m1 = hd ? 0xFFFFFFFFu : 0u;
  const unsigned sg0 = 0x80008000u & m0, sg1 = 0x80008000u & m1;
  const unsigned halfmask = h ? 0u : 0xFFFFFFFFu;
  const float onesC = (r32 == 16 || r32 == 20) ? 1.0f : 0.0f;
  const int eq_off = (r32 >> 1) * 32 + 16 * h;
  const int st_off = 16 * r32 + 8 * h;

  f32x16 zero16;
#pragma unroll
  for (int i = 0; i < 16; ++i) zero16[i] = 0.f;

  for (int bi = 0; bi < NB * REPEAT; ++bi) {
    const int b = wave * NB + (bi % NB);
    if (b >= nbatch) break;
    const __amdgpu_buffer_rsrc_t rs_e =
        __builtin_amdgcn_make_buffer_rsrc((void*)(e + (size_t)b * (F_ * D_)), (short)0, F_ * D_ * 2, 0x00020000);
    const __amdgpu_buffer_rsrc_t rs_o =
        __builtin_amdgcn_make_buffer_rsrc((void*)(out + (size_t)b * (F_ * D_)), (short)0, F_ * D_ * 2, 0x00020000);
#define LOAD_EQ(t) __builtin_amdgcn_raw_buffer_load_b128(rs_e, eq_off + 512 * (t), 0, 0)

    u32x4 Kf[4];
    u32x4 Vf[7];
    u32x4 Akm;
    u32x4 eq_cur = LOAD_EQ(0);
    u32x4 eq_n1 = LOAD_EQ(1);
    unsigned touch = 0;
    if (bi + 1 < NB) {
      const __amdgpu_buffer_rsrc_t rs_n = __builtin_amdgcn_make_buffer_rsrc(
          (void*)(e + (size_t)(b + 1) * (F_ * D_)), (short)0, (b + 1 < nbatch) ? F_ * D_ * 2 : 0, 0x00020000);
      touch = __builtin_amdgcn_raw_buffer_load_b32(rs_n, lane * 64, 0, 0);
    }
    {
      float mxk[8];
#pragma unroll
      for (int j = 0; j < 8; ++j) mxk[j] = 0.f;
      u32x4 ea[4];
#pragma unroll
      for (int tt = 0; tt < 4; ++tt)
        ea[tt] = __builtin_amdgcn_raw_buffer_load_b128(rs_e, (32 * tt + r32) * 32 + 16 * h, 0, 0);
      SB();
      if (bi == 0) asm volatile("" :: "v"(touch0));
      f32x16 oc;
#pragma unroll
      for (int i = 0; i < 16; ++i) oc[i] = onesC;
#pragma unroll
      for (int tt = 0; tt < 4; ++tt) {
        f32x16 dk = mfma32(Aqk, ea[tt], zero16);
        f32x16 dv = mfma32(ea[tt], Bv, oc);
#pragma unroll
        for (int i = 0; i < 4; ++i) Kf[tt][i] = pk_bf16(dk[8 + 2 * i], dk[9 + 2 * i]);
#pragma unroll
        for (int j = 0; j < 8; ++j) mxk[j] = fmaxf(mxk[j], fabsf(dk[8 + j]));
#pragma unroll
        for (int i = 0; i < 4; ++i) Vf[2 * tt][i] = pk_bf16(dv[2 * i], dv[2 * i + 1]);
        if (tt < 3) {
#pragma unroll
          for (int i = 0; i < 4; ++i) Vf[2 * tt + 1][i] = pk_bf16(dv[8 + 2 * i], dv[9 + 2 * i]);
        }
      }
#pragma unroll
      for (int i = 0; i < 4; ++i) Akm[i] = pk_bf16(mxk[2 * i], mxk[2 * i + 1]);
#pragma unroll
      for (int i = 0; i < 4; ++i) {
        Akm[i] = pkmax_u16(Akm[i], (unsigned)__builtin_amdgcn_ds_swizzle((int)Akm[i], 0x041F));
        Akm[i] = pkmax_u16(Akm[i], (unsigned)__builtin_amdgcn_ds_swizzle((int)Akm[i], 0x081F));
        Akm[i] = pkmax_u16(Akm[i], (unsigned)__builtin_amdgcn_ds_swizzle((int)Akm[i], 0x101F));
        Akm[i] = pkmax_u16(Akm[i], (unsigned)__builtin_amdgcn_ds_swizzle((int)Akm[i], 0x201F));
        Akm[i] = pkmax_u16(Akm[i], (unsigned)__builtin_amdgcn_ds_swizzle((int)Akm[i], 0x401F));
      }
    }

    u32x4 Qs;
    f32x16 Cb, S0;
    {
      f32x16 dq = mfma32(Aqk, eq_cur, zero16);
      unsigned q0 = pk_bf16(dq[0], dq[1]), q1 = pk_bf16(dq[2], dq[3]);
      unsigned q2 = pk_bf16(dq[4], dq[5]), q3 = pk_bf16(dq[6], dq[7]);
      Qs[0] = q0 & m0; Qs[1] = q1 & m0; Qs[2] = q2 & m1; Qs[3] = q3 & m1;
      u32x4 Qa;
      Qa[0] = Qs[0] | sg0; Qa[1] = Qs[1] | sg0; Qa[2] = Qs[2] | sg1; Qa[3] = Qs[3] | sg1;
      Cb = mfma32(Akm, Qa, zero16);
      S0 = mfma32(Kf[0], Qs, Cb);
    }
    f32x16 O = zero16;
    O[8] = 1.0f;
    u32x4 eq_prev = eq_cur;
    unsigned failmask = 0;

#define EP1()                                                   \
    {                                                           \
      const float l = O[8];                                     \
      const float rl = __builtin_amdgcn_rcpf(l);                \
      if (__builtin_amdgcn_ballot_w64(!(l >= 1e-10f)) != 0ull)  \
        failmask |= (1u << (tp < 0 ? 0 : tp));                                 \
      _Pragma("unroll") for (int i = 0; i < 8; ++i) O[i] *= rl; \
      O = mfma32(Ares, eq_prev, O);                             \
    }
#define EP2()                                                                   \
    {                                                                           \
      float v0 = hd ? O[4] : O[0], v1 = hd ? O[5] : O[1];                       \
      float v2 = hd ? O[6] : O[2], v3 = hd ? O[7] : O[3];                       \
      u32x2 ov;                                                                 \
      ov[0] = pk_relu_bf16(pk_bf16(v0, v1));                                    \
      ov[1] = pk_relu_bf16(pk_bf16(v2, v3));                                    \
      __builtin_amdgcn_raw_buffer_store_b64(ov, rs_o, st_off + 512 * tp, 0, 0); \
    }

#pragma unroll
    for (int t = 0; t < 7; ++t) {
      const int tp = t - 1;
      u32x4 eq_n2 = LOAD_EQ(t + 2);
      if (t == 0) { if (bi == 0) __builtin_amdgcn_s_setprio(3); else __builtin_amdgcn_s_setprio(1); }
      if (t == 4) { if (bi == 0) __builtin_amdgcn_s_setprio(2); else __builtin_amdgcn_s_setprio(0); }
      float p[16];
      u32x4 pa, pb;
      f32x16 S1, S2, S3;
#pragma unroll
      for (int i = 0; i < 4; ++i) p[i] = EXP2(S0[i]);
      S1 = mfma32(Kf[1], Qs, Cb);
#pragma unroll
      for (int i = 4; i < 8; ++i) p[i] = EXP2(S0[i]);
      EP1();
#pragma unroll
      for (int i = 0; i < 4; ++i) pa[i] = pk_bf16(p[2 * i], p[2 * i + 1]);
#pragma unroll
      for (int i = 8; i < 16; ++i) p[i] = EXP2(S0[i]);
      EP2();
      O = mfma32(Vf[0], pa, zero16);
#pragma unroll
      for (int i = 0; i < 4; ++i) pb[i] = pk_bf16(p[8 + 2 * i], p[9 + 2 * i]);
#pragma unroll
      for (int i = 0; i < 4; ++i) p[i] = EXP2(S1[i]);
      O = mfma32(Vf[1], pb, O);
#pragma unroll
      for (int i = 4; i < 8; ++i) p[i] = EXP2(S1[i]);
      S2 = mfma32(Kf[2], Qs, Cb);
#pragma unroll
      for (int i = 0; i < 4; ++i) pa[i] = pk_bf16(p[2 * i], p[2 * i + 1]);
#pragma unroll
      for (int i = 8; i < 12; ++i) p[i] = EXP2(S1[i]);
      O = mfma32(Vf[2], pa, O);
#pragma unroll
      for (int i = 12; i < 16; ++i) p[i] = EXP2(S1[i]);
      f32x16 dq = mfma32(Aqk, eq_n1, zero16);
#pragma unroll
      for (int i = 0; i < 4; ++i) pb[i] = pk_bf16(p[8 + 2 * i], p[9 + 2 * i]);
#pragma unroll
      for (int i = 0; i < 4; ++i) p[i] = EXP2(S2[i]);
      O = mfma32(Vf[3], pb, O);
#pragma unroll
      for (int i = 4; i < 8; ++i) p[i] = EXP2(S2[i]);
      S3 = mfma32(Kf[3], Qs, Cb);
      u32x4 Qsn, Qa;
      {
#pragma unroll
        for (int i = 0; i < 4; ++i) pa[i] = pk_bf16(p[2 * i], p[2 * i + 1]);
#pragma unroll
        for (int i = 8; i < 10; ++i) p[i] = EXP2(S2[i]);
        unsigned q0 = pk_bf16(dq[0], dq[1]), q1 = pk_bf16(dq[2], dq[3]);
        unsigned q2 = pk_bf16(dq[4], dq[5]), q3 = pk_bf16(dq[6], dq[7]);
        Qsn[0] = q0 & m0; Qsn[1] = q1 & m0; Qsn[2] = q2 & m1; Qsn[3] = q3 & m1;
        Qa[0] = Qsn[0] | sg0; Qa[1] = Qsn[1] | sg0; Qa[2] = Qsn[2] | sg1; Qa[3] = Qsn[3] | sg1;
      }
      O = mfma32(Vf[4], pa, O);
#pragma unroll
      for (int i = 10; i < 14; ++i) p[i] = EXP2(S2[i]);
      f32x16 Cbn = mfma32(Akm, Qa, zero16);
#pragma unroll
      for (int i = 14; i < 16; ++i) p[i] = EXP2(S2[i]);
#pragma unroll
      for (int i = 0; i < 4; ++i) pb[i] = pk_bf16(p[8 + 2 * i], p[9 + 2 * i]);
#pragma unroll
      for (int i = 0; i < 2; ++i) p[i] = EXP2(S3[i]);
      O = mfma32(Vf[5], pb, O);
#pragma unroll
      for (int i = 2; i < 4; ++i) p[i] = EXP2(S3[i]);
      pa[0] = pk_bf16(p[0], p[1]) & halfmask;
      pa[1] = pk_bf16(p[2], p[3]) & halfmask;
      pa[2] = 0u; pa[3] = 0u;
      S0 = mfma32(Kf[0], Qsn, Cbn);
      O = mfma32(Vf[6], pa, O);
#pragma unroll
      for (int g = 0; g < 13; ++g) {
        __builtin_amdgcn_sched_group_barrier(0x400, 4, 0);
        __builtin_amdgcn_sched_group_barrier(0x8, 1, 0);
      }
      __builtin_amdgcn_sched_group_barrier(0x8, 1, 0);
      Qs = Qsn;
      Cb = Cbn;
      eq_prev = eq_cur;
      eq_cur = eq_n1;
      eq_n1 = eq_n2;
    }
    asm volatile("" :: "v"(touch));
    {
      const int tp = 6;
      EP1();
      EP2();
    }
#undef EP1
#undef EP2

    if (failmask) {
      for (int t = 0; t < 7; ++t) {
        if (!((failmask >> t) & 1u)) continue;
        u32x4 eq = LOAD_EQ(t);
        f32x16 dq = mfma32(Aqk, eq, zero16);
        u32x4 Qx;
        Qx[0] = pk_bf16(dq[0], dq[1]) & m0; Qx[1] = pk_bf16(dq[2], dq[3]) & m0;
        Qx[2] = pk_bf16(dq[4], dq[5]) & m1; Qx[3] = pk_bf16(dq[6], dq[7]) & m1;
        float mx = -INFINITY;
#pragma unroll
        for (int kt = 0; kt < 4; ++kt) {
          f32x16 S = mfma32(Kf[kt], Qx, zero16);
          if (kt < 3) {
#pragma unroll
            for (int i = 0; i < 16; ++i) mx = fmaxf(mx, S[i]);
          } else {
#pragma unroll
            for (int i = 0; i < 4; ++i) mx = fmaxf(mx, h ? -INFINITY : S[i]);
          }
        }
        mx = fmaxf(mx, __shfl_xor(mx, 32));
        f32x16 Cx;
#pragma unroll
        for (int i = 0; i < 16; ++i) Cx[i] = -mx;
        f32x16 Ox = zero16;
#pragma unroll
        for (int kt = 0; kt < 4; ++kt) {
          f32x16 S = mfma32(Kf[kt], Qx, Cx);
          if (kt < 3) {
            u32x4 p0, p1;
#pragma unroll
            for (int i = 0; i < 4; ++i) {
              p0[i] = pk_bf16(EXP2(S[2 * i]), EXP2(S[2 * i + 1]));
              p1[i] = pk_bf16(EXP2(S[8 + 2 * i]), EXP2(S[9 + 2 * i]));
            }
            Ox = mfma32(Vf[2 * kt], p0, Ox);
            Ox = mfma32(Vf[2 * kt + 1], p1, Ox);
          } else {
            u32x4 p0;
            p0[0] = pk_bf16(EXP2(S[0]), EXP2(S[1])) & halfmask;
            p0[1] = pk_bf16(EXP2(S[2]), EXP2(S[3])) & halfmask;
            p0[2] = 0u; p0[3] = 0u;
            Ox = mfma32(Vf[6], p0, Ox);
          }
        }
        const float rl = __builtin_amdgcn_rcpf(Ox[8]);
#pragma unroll
        for (int i = 0; i < 8; ++i) Ox[i] *= rl;
        Ox = mfma32(Ares, eq, Ox);
        float v0 = hd ? Ox[4] : Ox[0], v1 = hd ? Ox[5] : Ox[1];
        float v2 = hd ? Ox[6] : Ox[2], v3 = hd ? Ox[7] : Ox[3];
        u32x2 ov;
        ov[0] = pk_relu_bf16(pk_bf16(v0, v1));
        ov[1] = pk_relu_bf16(pk_bf16(v2, v3));
        __builtin_amdgcn_raw_buffer_store_b64(ov, rs_o, st_off + 512 * t, 0, 0);
      }
    }
#undef LOAD_EQ
  }
}

extern "C" void kernel_launch(void* const* d_in, const int* in_sizes, int n_in, void* d_out, int out_size, void* d_ws,
                              size_t ws_size, hipStream_t stream) {
  const unsigned short* e = (const unsigned short*)d_in[0];
  const unsigned short* wq = (const unsigned short*)d_in[1];
  const unsigned short* wk = (const unsigned short*)d_in[2];
  const unsigned short* wv = (const unsigned short*)d_in[3];
  const unsigned short* wres = (const unsigned short*)d_in[4];
  const int nbatch = in_sizes[0] / (F_ * D_);
  constexpr int NB = 2;
  const int nwaves = (nbatch + NB - 1) / NB;
  const int nblocks = (nwaves + 3) / 4;
  autoint_kernel<NB><<<nblocks, 256, 0, stream>>>(e, wq, wk, wv, wres, (unsigned short*)d_out, nbatch);
}
```

```cpp
#include <hip/hip_runtime.h>
#include <hip/hip_bf16.h>
#include <stdint.h>

typedef __attribute__((ext_vector_type(8))) short bf16x8;
typedef __attribute__((ext_vector_type(16))) float f32x16;
typedef __attribute__((ext_vector_type(4))) unsigned u32x4;
typedef __attribute__((ext_vector_type(2))) unsigned u32x2;
typedef __attribute__((ext_vector_type(2))) __bf16 bf16x2;
typedef __attribute__((ext_vector_type(2))) unsigned short u16x2;
typedef __attribute__((ext_vector_type(2))) short s16x2;

#define DEV __device__ __forceinline__
#define F_ 100
#define D_ 16
#define SB() __builtin_amdgcn_sched_barrier(0)
#define REPEAT 1

DEV unsigned pk_bf16(float a, float b) {
  bf16x2 v;
  v[0] = (__bf16)a;
  v[1] = (__bf16)b;
  return __builtin_bit_cast(unsigned, v);
}
DEV f32x16 mfma32(u32x4 a, u32x4 b, f32x16 c) {
  return __builtin_amdgcn_mfma_f32_32x32x16_bf16(__builtin_bit_cast(bf16x8, a), __builtin_bit_cast(bf16x8, b), c, 0, 0, 0);
}
DEV float bfbits2f(unsigned short h) { return __uint_as_float(((unsigned)h) << 16); }
DEV unsigned short f2bfbits(float x) {
  __bf16 v = (__bf16)x;
  return __builtin_bit_cast(unsigned short, v);
}
DEV unsigned pkmax_u16(unsigned a, unsigned b) {
  u16x2 x = __builtin_bit_cast(u16x2, a), y = __builtin_bit_cast(u16x2, b);
  return __builtin_bit_cast(unsigned, __builtin_elementwise_max(x, y));
}
DEV unsigned pk_relu_bf16(unsigned a) {
  s16x2 x = __builtin_bit_cast(s16x2, a);
  s16x2 z = {0, 0};
  return __builtin_bit_cast(unsigned, __builtin_elementwise_max(x, z));
}
DEV unsigned cvtpk_asm(float lo, float hi) {
  unsigned r;
  asm("v_cvt_pk_bf16_f32 %0, %1, %2" : "=v"(r) : "v"(lo), "v"(hi));
  return r;
}
#define EXP2(x) __builtin_amdgcn_exp2f(x)

template <int NB>
__global__ __launch_bounds__(256, 2) void autoint_kernel(const unsigned short* __restrict__ e,
                                                         const unsigned short* __restrict__ wq,
                                                         const unsigned short* __restrict__ wk,
                                                         const unsigned short* __restrict__ wv,
                                                         const unsigned short* __restrict__ wres,
                                                         unsigned short* __restrict__ out, int nbatch) {
  const int lane = threadIdx.x & 63;
  const int r32 = lane & 31;
  const int h = lane >> 5;
  const int hd = r32 & 1;
  const int wave = __builtin_amdgcn_readfirstlane(blockIdx.x * 4 + (threadIdx.x >> 6));

  const float CSC = 0.35355339059327373f * 1.4426950408889634f;

  unsigned touch0;
  {
    const int b0 = wave * NB;
    const __amdgpu_buffer_rsrc_t rs0 =
        __builtin_amdgcn_make_buffer_rsrc((void*)(e + (size_t)b0 * (F_ * D_)), (short)0, (b0 < nbatch) ? F_ * D_ * 2 : 0, 0x00020000);
    touch0 = __builtin_amdgcn_raw_buffer_load_b32(rs0, lane * 64, 0, 0);
  }
  SB();
  u32x4 Aqk, Bv, Ares;
  {
    unsigned short tq[8], tv[8], tr[8];
#pragma unroll
    for (int j = 0; j < 8; ++j) {
      const int d = 8 * h + j;
      const int c = r32 & 15;
      const int idx = ((c >> 3) * D_ + d) * 8 + (c & 7);
      unsigned short aq = wq[idx], ak = wk[idx], av = wv[idx], ar = wres[d * D_ + c];
      unsigned short sq = f2bfbits(bfbits2f(aq) * CSC);
      tq[j] = (r32 < 16) ? sq : ak;
      tv[j] = (r32 < 16) ? av : (unsigned short)0;
      tr[j] = (r32 < 16) ? ar : (unsigned short)0;
    }
#pragma unroll
    for (int i = 0; i < 4; ++i) {
      Aqk[i] = (unsigned)tq[2 * i] | ((unsigned)tq[2 * i + 1] << 16);
      Bv[i] = (unsigned)tv[2 * i] | ((unsigned)tv[2 * i + 1] << 16);
      Ares[i] = (unsigned)tr[2 * i] | ((unsigned)tr[2 * i + 1] << 16);
    }
  }
  const unsigned m0 = hd ? 0u : 0xFFFFFFFFu;
  const unsigned m1 = hd ? 0xFFFFFFFFu : 0u;
  const unsigned sg0 = 0x80008000u & m0, sg1 = 0x80008000u & m1;
  const unsigned halfmask = h ? 0u : 0xFFFFFFFFu;
  const float onesC = (r32 == 16 || r32 == 20) ? 1.0f : 0.0f;
  const int eq_off = (r32 >> 1) * 32 + 16 * h;
  const int st_off = 16 * r32 + 8 * h;

  f32x16 zero16;
#pragma unroll
  for (int i = 0; i < 16; ++i) zero16[i] = 0.f;

  for (int bi = 0; bi < NB * REPEAT; ++bi) {
    const int b = wave * NB + (bi % NB);
    if (b >= nbatch) break;
    const __amdgpu_buffer_rsrc_t rs_e =
        __builtin_amdgcn_make_buffer_rsrc((void*)(e + (size_t)b * (F_ * D_)), (short)0, F_ * D_ * 2, 0x00020000);
    const __amdgpu_buffer_rsrc_t rs_o =
        __builtin_amdgcn_make_buffer_rsrc((void*)(out + (size_t)b * (F_ * D_)), (short)0, F_ * D_ * 2, 0x00020000);
#define LOAD_EQ(t) __builtin_amdgcn_raw_buffer_load_b128(rs_e, eq_off + 512 * (t), 0, 0)

    u32x4 Kf[4];
    u32x4 Vf[7];
    u32x4 Akm;
    u32x4 eq_cur = LOAD_EQ(0);
    u32x4 eq_n1 = LOAD_EQ(1);
    unsigned touch = 0;
    if (bi + 1 < NB) {
      const __amdgpu_buffer_rsrc_t rs_n = __builtin_amdgcn_make_buffer_rsrc(
          (void*)(e + (size_t)(b + 1) * (F_ * D_)), (short)0, (b + 1 < nbatch) ? F_ * D_ * 2 : 0, 0x00020000);
      touch = __builtin_amdgcn_raw_buffer_load_b32(rs_n, lane * 64, 0, 0);
    }
    {
      float mxk[8];
#pragma unroll
      for (int j = 0; j < 8; ++j) mxk[j] = 0.f;
      u32x4 ea[4];
#pragma unroll
      for (int tt = 0; tt < 4; ++tt)
        ea[tt] = __builtin_amdgcn_raw_buffer_load_b128(rs_e, (32 * tt + r32) * 32 + 16 * h, 0, 0);
      SB();
      if (bi == 0) asm volatile("" :: "v"(touch0));
      f32x16 oc;
#pragma unroll
      for (int i = 0; i < 16; ++i) oc[i] = onesC;
#pragma unroll
      for (int tt = 0; tt < 4; ++tt) {
        f32x16 dk = mfma32(Aqk, ea[tt], zero16);
        f32x16 dv = mfma32(ea[tt], Bv, oc);
#pragma unroll
        for (int i = 0; i < 4; ++i) Kf[tt][i] = pk_bf16(dk[8 + 2 * i], dk[9 + 2 * i]);
#pragma unroll
        for (int j = 0; j < 8; ++j) mxk[j] = fmaxf(mxk[j], fabsf(dk[8 + j]));
#pragma unroll
        for (int i = 0; i < 4; ++i) Vf[2 * tt][i] = pk_bf16(dv[2 * i], dv[2 * i + 1]);
        if (tt < 3) {
#pragma unroll
          for (int i = 0; i < 4; ++i) Vf[2 * tt + 1][i] = pk_bf16(dv[8 + 2 * i], dv[9 + 2 * i]);
        }
      }
#pragma unroll
      for (int i = 0; i < 4; ++i) Akm[i] = pk_bf16(mxk[2 * i], mxk[2 * i + 1]);
#pragma unroll
      for (int i = 0; i < 4; ++i) {
        Akm[i] = pkmax_u16(Akm[i], (unsigned)__builtin_amdgcn_ds_swizzle((int)Akm[i], 0x041F));
        Akm[i] = pkmax_u16(Akm[i], (unsigned)__builtin_amdgcn_ds_swizzle((int)Akm[i], 0x081F));
        Akm[i] = pkmax_u16(Akm[i], (unsigned)__builtin_amdgcn_ds_swizzle((int)Akm[i], 0x101F));
        Akm[i] = pkmax_u16(Akm[i], (unsigned)__builtin_amdgcn_ds_swizzle((int)Akm[i], 0x201F));
        Akm[i] = pkmax_u16(Akm[i], (unsigned)__builtin_amdgcn_ds_swizzle((int)Akm[i], 0x401F));
      }
    }

    u32x4 Qs;
    f32x16 Cb, S0;
    {
      f32x16 dq = mfma32(Aqk, eq_cur, zero16);
      unsigned q0 = pk_bf16(dq[0], dq[1]), q1 = pk_bf16(dq[2], dq[3]);
      unsigned q2 = pk_bf16(dq[4], dq[5]), q3 = pk_bf16(dq[6], dq[7]);
      Qs[0] = q0 & m0; Qs[1] = q1 & m0; Qs[2] = q2 & m1; Qs[3] = q3 & m1;
      u32x4 Qa;
      Qa[0] = Qs[0] | sg0; Qa[1] = Qs[1] | sg0; Qa[2] = Qs[2] | sg1; Qa[3] = Qs[3] | sg1;
      Cb = mfma32(Akm, Qa, zero16);
      S0 = mfma32(Kf[0], Qs, Cb);
    }
    f32x16 O = zero16;
    O[8] = 1.0f;
    f32x16 R = zero16;
    u32x4 eq_prev = eq_cur;
    unsigned failmask = 0;

#define EP1()                                                   \
    {                                                           \
      const float l = O[8];                                     \
      const float rl = __builtin_amdgcn_rcpf(l);                \
      if (__builtin_amdgcn_ballot_w64(!(l >= 1e-10f)) != 0ull)  \
        failmask |= (1u << (tp < 0 ? 0 : tp));                                 \
      _Pragma("unroll") for (int i = 0; i < 8; ++i) R[i] = O[i] * rl; \
      R = mfma32(Ares, eq_prev, R);                             \
    }
#define EP2()                                                                   \
    {                                                                           \
      float v0 = hd ? R[4] : R[0], v1 = hd ? R[5] : R[1];                       \
      float v2 = hd ? R[6] : R[2], v3 = hd ? R[7] : R[3];                       \
      u32x2 ov;                                                                 \
      ov[0] = pk_relu_bf16(cvtpk_asm(v0, v1));                                  \
      ov[1] = pk_relu_bf16(cvtpk_asm(v2, v3));                                  \
      __builtin_amdgcn_raw_buffer_store_b64(ov, rs_o, st_off + 512 * tp, 0, 0); \
    }

#pragma unroll
    for (int t = 0; t < 7; ++t) {
      const int tp = t - 1;
      u32x4 eq_n2 = LOAD_EQ(t + 2);
      if (t == 0) { if (bi == 0) __builtin_amdgcn_s_setprio(3); else __builtin_amdgcn_s_setprio(1); }
      if (t == 4) { if (bi == 0) __builtin_amdgcn_s_setprio(2); else __builtin_amdgcn_s_setprio(0); }
      float p[16];
      u32x4 pa, pb;
      f32x16 S1, S2, S3;
#pragma unroll
      for (int i = 0; i < 4; ++i) p[i] = EXP2(S0[i]);
      S1 = mfma32(Kf[1], Qs, Cb);
#pragma unroll
      for (int i = 4; i < 8; ++i) p[i] = EXP2(S0[i]);
      EP1();
#pragma unroll
      for (int i = 0; i < 4; ++i) pa[i] = pk_bf16(p[2 * i], p[2 * i + 1]);
#pragma unroll
      for (int i = 8; i < 16; ++i) p[i] = EXP2(S0[i]);
      EP2();
      O = mfma32(Vf[0], pa, zero16);
#pragma unroll
      for (int i = 0; i < 4; ++i) pb[i] = pk_bf16(p[8 + 2 * i], p[9 + 2 * i]);
#pragma unroll
      for (int i = 0; i < 4; ++i) p[i] = EXP2(S1[i]);
      O = mfma32(Vf[1], pb, O);
#pragma unroll
      for (int i = 4; i < 8; ++i) p[i] = EXP2(S1[i]);
      S2 = mfma32(Kf[2], Qs, Cb);
#pragma unroll
      for (int i = 0; i < 4; ++i) pa[i] = pk_bf16(p[2 * i], p[2 * i + 1]);
#pragma unroll
      for (int i = 8; i < 12; ++i) p[i] = EXP2(S1[i]);
      O = mfma32(Vf[2], pa, O);
#pragma unroll
      for (int i = 12; i < 16; ++i) p[i] = EXP2(S1[i]);
      f32x16 dq = mfma32(Aqk, eq_n1, zero16);
#pragma unroll
      for (int i = 0; i < 4; ++i) pb[i] = pk_bf16(p[8 + 2 * i], p[9 + 2 * i]);
#pragma unroll
      for (int i = 0; i < 4; ++i) p[i] = EXP2(S2[i]);
      O = mfma32(Vf[3], pb, O);
#pragma unroll
      for (int i = 4; i < 8; ++i) p[i] = EXP2(S2[i]);
      S3 = mfma32(Kf[3], Qs, Cb);
      u32x4 Qsn, Qa;
      {
#pragma unroll
        for (int i = 0; i < 4; ++i) pa[i] = pk_bf16(p[2 * i], p[2 * i + 1]);
#pragma unroll
        for (int i = 8; i < 10; ++i) p[i] = EXP2(S2[i]);
        unsigned q0 = pk_bf16(dq[0], dq[1]), q1 = pk_bf16(dq[2], dq[3]);
        unsigned q2 = pk_bf16(dq[4], dq[5]), q3 = pk_bf16(dq[6], dq[7]);
        Qsn[0] = q0 & m0; Qsn[1] = q1 & m0; Qsn[2] = q2 & m1; Qsn[3] = q3 & m1;
        Qa[0] = Qsn[0] | sg0; Qa[1] = Qsn[1] | sg0; Qa[2] = Qsn[2] | sg1; Qa[3] = Qsn[3] | sg1;
      }
      O = mfma32(Vf[4], pa, O);
#pragma unroll
      for (int i = 10; i < 14; ++i) p[i] = EXP2(S2[i]);
      f32x16 Cbn = mfma32(Akm, Qa, zero16);
#pragma unroll
      for (int i = 14; i < 16; ++i) p[i] = EXP2(S2[i]);
#pragma unroll
      for (int i = 0; i < 4; ++i) pb[i] = pk_bf16(p[8 + 2 * i], p[9 + 2 * i]);
#pragma unroll
      for (int i = 0; i < 2; ++i) p[i] = EXP2(S3[i]);
      O = mfma32(Vf[5], pb, O);
#pragma unroll
      for (int i = 2; i < 4; ++i) p[i] = EXP2(S3[i]);
      pa[0] = pk_bf16(p[0], p[1]) & halfmask;
      pa[1] = pk_bf16(p[2], p[3]) & halfmask;
      pa[2] = 0u; pa[3] = 0u;
      S0 = mfma32(Kf[0], Qsn, Cbn);
      O = mfma32(Vf[6], pa, O);
#pragma unroll
      for (int g = 0; g < 13; ++g) {
        __builtin_amdgcn_sched_group_barrier(0x400, 4, 0);
        __builtin_amdgcn_sched_group_barrier(0x8, 1, 0);
      }
      __builtin_amdgcn_sched_group_barrier(0x8, 1, 0);
      Qs = Qsn;
      Cb = Cbn;
      eq_prev = eq_cur;
      eq_cur = eq_n1;
      eq_n1 = eq_n2;
    }
    asm volatile("" :: "v"(touch));
    {
      const int tp = 6;
      EP1();
      EP2();
    }
#undef EP1
#undef EP2

    if (failmask) {
      for (int t = 0; t < 7; ++t) {
        if (!((failmask >> t) & 1u)) continue;
        u32x4 eq = LOAD_EQ(t);
        f32x16 dq = mfma32(Aqk, eq, zero16);
        u32x4 Qx;
        Qx[0] = pk_bf16(dq[0], dq[1]) & m0; Qx[1] = pk_bf16(dq[2], dq[3]) & m0;
        Qx[2] = pk_bf16(dq[4], dq[5]) & m1; Qx[3] = pk_bf16(dq[6], dq[7]) & m1;
        float mx = -INFINITY;
#pragma unroll
        for (int kt = 0; kt < 4; ++kt) {
          f32x16 S = mfma32(Kf[kt], Qx, zero16);
          if (kt < 3) {
#pragma unroll
            for (int i = 0; i < 16; ++i) mx = fmaxf(mx, S[i]);
          } else {
#pragma unroll
            for (int i = 0; i < 4; ++i) mx = fmaxf(mx, h ? -INFINITY : S[i]);
          }
        }
        mx = fmaxf(mx, __shfl_xor(mx, 32));
        f32x16 Cx;
#pragma unroll
        for (int i = 0; i < 16; ++i) Cx[i] = -mx;
        f32x16 Ox = zero16;
#pragma unroll
        for (int kt = 0; kt < 4; ++kt) {
          f32x16 S = mfma32(Kf[kt], Qx, Cx);
          if (kt < 3) {
            u32x4 p0, p1;
#pragma unroll
            for (int i = 0; i < 4; ++i) {
              p0[i] = pk_bf16(EXP2(S[2 * i]), EXP2(S[2 * i + 1]));
              p1[i] = pk_bf16(EXP2(S[8 + 2 * i]), EXP2(S[9 + 2 * i]));
            }
            Ox = mfma32(Vf[2 * kt], p0, Ox);
            Ox = mfma32(Vf[2 * kt + 1], p1, Ox);
          } else {
            u32x4 p0;
            p0[0] = pk_bf16(EXP2(S[0]), EXP2(S[1])) & halfmask;
            p0[1] = pk_bf16(EXP2(S[2]), EXP2(S[3])) & halfmask;
            p0[2] = 0u; p0[3] = 0u;
            Ox = mfma32(Vf[6], p0, Ox);
          }
        }
        const float rl = __builtin_amdgcn_rcpf(Ox[8]);
#pragma unroll
        for (int i = 0; i < 8; ++i) Ox[i] *= rl;
        Ox = mfma32(Ares, eq, Ox);
        float v0 = hd ? Ox[4] : Ox[0], v1 = hd ? Ox[5] : Ox[1];
        float v2 = hd ? Ox[6] : Ox[2], v3 = hd ? Ox[7] : Ox[3];
        u32x2 ov;
        ov[0] = pk_relu_bf16(pk_bf16(v0, v1));
        ov[1] = pk_relu_bf16(pk_bf16(v2, v3));
        __builtin_amdgcn_raw_buffer_store_b64(ov, rs_o, st_off + 512 * t, 0, 0);
      }
    }
#undef LOAD_EQ
  }
}

extern "C" void kernel_launch(void* const* d_in, const int* in_sizes, int n_in, void* d_out, int out_size, void* d_ws,
                              size_t ws_size, hipStream_t stream) {
  const unsigned short* e = (const unsigned short*)d_in[0];
  const unsigned short* wq = (const unsigned short*)d_in[1];
  const unsigned short* wk = (const unsigned short*)d_in[2];
  const unsigned short* wv = (const unsigned short*)d_in[3];
  const unsigned short* wres = (const unsigned short*)d_in[4];
  const int nbatch = in_sizes[0] / (F_ * D_);
  constexpr int NB = 2;
  const int nwaves = (nbatch + NB - 1) / NB;
  const int nblocks = (nwaves + 3) / 4;
  autoint_kernel<NB><<<nblocks, 256, 0, stream>>>(e, wq, wk, wv, wres, (unsigned short*)d_out, nbatch);
}
```

```cpp
#include <hip/hip_runtime.h>
#include <hip/hip_bf16.h>
#include <stdint.h>

typedef __attribute__((ext_vector_type(8))) short bf16x8;
typedef __attribute__((ext_vector_type(16))) float f32x16;
typedef __attribute__((ext_vector_type(4))) unsigned u32x4;
typedef __attribute__((ext_vector_type(2))) unsigned u32x2;
typedef __attribute__((ext_vector_type(2))) __bf16 bf16x2;
typedef __attribute__((ext_vector_type(2))) unsigned short u16x2;
typedef __attribute__((ext_vector_type(2))) short s16x2;

#define DEV __device__ __forceinline__
#define F_ 100
#define D_ 16
#define SB() __builtin_amdgcn_sched_barrier(0)
#define REPEAT 1

DEV unsigned pk_bf16(float a, float b) {
  bf16x2 v;
  v[0] = (__bf16)a;
  v[1] = (__bf16)b;
  return __builtin_bit_cast(unsigned, v);
}
DEV f32x16 mfma32(u32x4 a, u32x4 b, f32x16 c) {
  return __builtin_amdgcn_mfma_f32_32x32x16_bf16(__builtin_bit_cast(bf16x8, a), __builtin_bit_cast(bf16x8, b), c, 0, 0, 0);
}
DEV float bfbits2f(unsigned short h) { return __uint_as_float(((unsigned)h) << 16); }
DEV unsigned short f2bfbits(float x) {
  __bf16 v = (__bf16)x;
  return __builtin_bit_cast(unsigned short, v);
}
DEV unsigned pkmax_u16(unsigned a, unsigned b) {
  u16x2 x = __builtin_bit_cast(u16x2, a), y = __builtin_bit_cast(u16x2, b);
  return __builtin_bit_cast(unsigned, __builtin_elementwise_max(x, y));
}
DEV unsigned pk_relu_bf16(unsigned a) {
  s16x2 x = __builtin_bit_cast(s16x2, a);
  s16x2 z = {0, 0};
  return __builtin_bit_cast(unsigned, __builtin_elementwise_max(x, z));
}
DEV unsigned cvtpk_asm(float lo, float hi) {
  unsigned r;
  asm("v_cvt_pk_bf16_f32 %0, %1, %2" : "=v"(r) : "v"(lo), "v"(hi));
  return r;
}
#define EXP2(x) __builtin_amdgcn_exp2f(x)

template <int NB>
__global__ __launch_bounds__(256, 2) void autoint_kernel(const unsigned short* __restrict__ e,
                                                         const unsigned short* __restrict__ wq,
                                                         const unsigned short* __restrict__ wk,
                                                         const unsigned short* __restrict__ wv,
                                                         const unsigned short* __restrict__ wres,
                                                         unsigned short* __restrict__ out, int nbatch) {
  const int lane = threadIdx.x & 63;
  const int r32 = lane & 31;
  const int h = lane >> 5;
  const int hd = r32 & 1;
  const int wave = __builtin_amdgcn_readfirstlane(blockIdx.x * 4 + (threadIdx.x >> 6));

  const float CSC = 0.35355339059327373f * 1.4426950408889634f;

  unsigned touch0;
  {
    const int b0 = wave * NB;
    const __amdgpu_buffer_rsrc_t rs0 =
        __builtin_amdgcn_make_buffer_rsrc((void*)(e + (size_t)b0 * (F_ * D_)), (short)0, (b0 < nbatch) ? F_ * D_ * 2 : 0, 0x00020000);
    touch0 = __builtin_amdgcn_raw_buffer_load_b32(rs0, lane * 64, 0, 0);
  }
  SB();
  u32x4 Aqk, Bv, Ares;
  {
    unsigned short tq[8], tv[8], tr[8];
#pragma unroll
    for (int j = 0; j < 8; ++j) {
      const int d = 8 * h + j;
      const int c = r32 & 15;
      const int idx = ((c >> 3) * D_ + d) * 8 + (c & 7);
      unsigned short aq = wq[idx], ak = wk[idx], av = wv[idx], ar = wres[d * D_ + c];
      unsigned short sq = f2bfbits(bfbits2f(aq) * CSC);
      tq[j] = (r32 < 16) ? sq : ak;
      tv[j] = (r32 < 16) ? av : (unsigned short)0;
      tr[j] = (r32 < 16) ? ar : (unsigned short)0;
    }
#pragma unroll
    for (int i = 0; i < 4; ++i) {
      Aqk[i] = (unsigned)tq[2 * i] | ((unsigned)tq[2 * i + 1] << 16);
      Bv[i] = (unsigned)tv[2 * i] | ((unsigned)tv[2 * i + 1] << 16);
      Ares[i] = (unsigned)tr[2 * i] | ((unsigned)tr[2 * i + 1] << 16);
    }
  }
  const unsigned m0 = hd ? 0u : 0xFFFFFFFFu;
  const unsigned m1 = hd ? 0xFFFFFFFFu : 0u;
  const unsigned sg0 = 0x80008000u & m0, sg1 = 0x80008000u & m1;
  const unsigned halfmask = h ? 0u : 0xFFFFFFFFu;
  const float onesC = (r32 == 16 || r32 == 20) ? 1.0f : 0.0f;
  const int eq_off = (r32 >> 1) * 32 + 16 * h;
  const int st_off = 16 * r32 + 8 * h;

  f32x16 zero16;
#pragma unroll
  for (int i = 0; i < 16; ++i) zero16[i] = 0.f;

  for (int bi = 0; bi < NB * REPEAT; ++bi) {
    const int b = wave * NB + (bi % NB);
    if (b >= nbatch) break;
    const __amdgpu_buffer_rsrc_t rs_e =
        __builtin_amdgcn_make_buffer_rsrc((void*)(e + (size_t)b * (F_ * D_)), (short)0, F_ * D_ * 2, 0x00020000);
    const __amdgpu_buffer_rsrc_t rs_o =
        __builtin_amdgcn_make_buffer_rsrc((void*)(out + (size_t)b * (F_ * D_)), (short)0, F_ * D_ * 2, 0x00020000);
#define LOAD_EQ(t) __builtin_amdgcn_raw_buffer_load_b128(rs_e, eq_off + 512 * (t), 0, 0)

    u32x4 Kf[4];
    u32x4 Vf[7];
    u32x4 Akm;
    u32x4 eq_cur = LOAD_EQ(0);
    u32x4 eq_n1 = LOAD_EQ(1);
    unsigned touch = 0;
    {
      float mxk[8];
#pragma unroll
      for (int j = 0; j < 8; ++j) mxk[j] = 0.f;
      u32x4 ea[4];
#pragma unroll
      for (int tt = 0; tt < 4; ++tt)
        ea[tt] = __builtin_amdgcn_raw_buffer_load_b128(rs_e, (32 * tt + r32) * 32 + 16 * h, 0, 0);
      SB();
      if (bi == 0) asm volatile("" :: "v"(touch0));
      f32x16 oc;
#pragma unroll
      for (int i = 0; i < 16; ++i) oc[i] = onesC;
#pragma unroll
      for (int tt = 0; tt < 4; ++tt) {
        f32x16 dk = mfma32(Aqk, ea[tt], zero16);
        f32x16 dv = mfma32(ea[tt], Bv, oc);
#pragma unroll
        for (int i = 0; i < 4; ++i) Kf[tt][i] = pk_bf16(dk[8 + 2 * i], dk[9 + 2 * i]);
#pragma unroll
        for (int j = 0; j < 8; ++j) mxk[j] = fmaxf(mxk[j], fabsf(dk[8 + j]));
#pragma unroll
        for (int i = 0; i < 4; ++i) Vf[2 * tt][i] = pk_bf16(dv[2 * i], dv[2 * i + 1]);
        if (tt < 3) {
#pragma unroll
          for (int i = 0; i < 4; ++i) Vf[2 * tt + 1][i] = pk_bf16(dv[8 + 2 * i], dv[9 + 2 * i]);
        }
      }
#pragma unroll
      for (int i = 0; i < 4; ++i) Akm[i] = pk_bf16(mxk[2 * i], mxk[2 * i + 1]);
#pragma unroll
      for (int i = 0; i < 4; ++i) {
        Akm[i] = pkmax_u16(Akm[i], (unsigned)__builtin_amdgcn_ds_swizzle((int)Akm[i], 0x041F));
        Akm[i] = pkmax_u16(Akm[i], (unsigned)__builtin_amdgcn_ds_swizzle((int)Akm[i], 0x081F));
        Akm[i] = pkmax_u16(Akm[i], (unsigned)__builtin_amdgcn_ds_swizzle((int)Akm[i], 0x101F));
        Akm[i] = pkmax_u16(Akm[i], (unsigned)__builtin_amdgcn_ds_swizzle((int)Akm[i], 0x201F));
        Akm[i] = pkmax_u16(Akm[i], (unsigned)__builtin_amdgcn_ds_swizzle((int)Akm[i], 0x401F));
      }
    }

    u32x4 Qs;
    f32x16 Cb, S0;
    {
      f32x16 dq = mfma32(Aqk, eq_cur, zero16);
      unsigned q0 = pk_bf16(dq[0], dq[1]), q1 = pk_bf16(dq[2], dq[3]);
      unsigned q2 = pk_bf16(dq[4], dq[5]), q3 = pk_bf16(dq[6], dq[7]);
      Qs[0] = q0 & m0; Qs[1] = q1 & m0; Qs[2] = q2 & m1; Qs[3] = q3 & m1;
      u32x4 Qa;
      Qa[0] = Qs[0] | sg0; Qa[1] = Qs[1] | sg0; Qa[2] = Qs[2] | sg1; Qa[3] = Qs[3] | sg1;
      Cb = mfma32(Akm, Qa, zero16);
      S0 = mfma32(Kf[0], Qs, Cb);
    }
    f32x16 O = zero16;
    O[8] = 1.0f;
    f32x16 R = zero16;
    u32x4 eq_prev = eq_cur;
    unsigned failmask = 0;

#define EP1()                                                   \
    {                                                           \
      const float l = O[8];                                     \
      const float rl = __builtin_amdgcn_rcpf(l);                \
      if (__builtin_amdgcn_ballot_w64(!(l >= 1e-10f)) != 0ull)  \
        failmask |= (1u << (tp < 0 ? 0 : tp));                                 \
      _Pragma("unroll") for (int i = 0; i < 8; ++i) R[i] = O[i] * rl; \
      R = mfma32(Ares, eq_prev, R);                             \
    }
#define EP2()                                                                   \
    {                                                                           \
      float v0 = hd ? R[4] : R[0], v1 = hd ? R[5] : R[1];                       \
      float v2 = hd ? R[6] : R[2], v3 = hd ? R[7] : R[3];                       \
      u32x2 ov;                                                                 \
      ov[0] = pk_relu_bf16(cvtpk_asm(v0, v1));                                  \
      ov[1] = pk_relu_bf16(cvtpk_asm(v2, v3));                                  \
      __builtin_amdgcn_raw_buffer_store_b64(ov, rs_o, st_off + 512 * tp, 0, 0); \
    }

#pragma unroll
    for (int t = 0; t < 7; ++t) {
      const int tp = t - 1;
      u32x4 eq_n2 = LOAD_EQ(t + 2);
      if (t == 3 && bi + 1 < NB) {
        const __amdgpu_buffer_rsrc_t rs_n = __builtin_amdgcn_make_buffer_rsrc(
            (void*)(e + (size_t)(b + 1) * (F_ * D_)), (short)0, (b + 1 < nbatch) ? F_ * D_ * 2 : 0, 0x00020000);
        touch = __builtin_amdgcn_raw_buffer_load_b32(rs_n, lane * 64, 0, 0);
      }
      if (t == 0) { if (bi == 0) __builtin_amdgcn_s_setprio(3); else __builtin_amdgcn_s_setprio(1); }
      if (t == 4) { if (bi == 0) __builtin_amdgcn_s_setprio(2); else __builtin_amdgcn_s_setprio(0); }
      float p[16];
      u32x4 pa, pb;
      f32x16 S1, S2, S3;
#pragma unroll
      for (int i = 0; i < 4; ++i) p[i] = EXP2(S0[i]);
      S1 = mfma32(Kf[1], Qs, Cb);
#pragma unroll
      for (int i = 4; i < 8; ++i) p[i] = EXP2(S0[i]);
      EP1();
#pragma unroll
      for (int i = 0; i < 4; ++i) pa[i] = pk_bf16(p[2 * i], p[2 * i + 1]);
#pragma unroll
      for (int i = 8; i < 16; ++i) p[i] = EXP2(S0[i]);
      EP2();
      O = mfma32(Vf[0], pa, zero16);
#pragma unroll
      for (int i = 0; i < 4; ++i) pb[i] = pk_bf16(p[8 + 2 * i], p[9 + 2 * i]);
#pragma unroll
      for (int i = 0; i < 4; ++i) p[i] = EXP2(S1[i]);
      O = mfma32(Vf[1], pb, O);
#pragma unroll
      for (int i = 4; i < 8; ++i) p[i] = EXP2(S1[i]);
      S2 = mfma32(Kf[2], Qs, Cb);
#pragma unroll
      for (int i = 0; i < 4; ++i) pa[i] = pk_bf16(p[2 * i], p[2 * i + 1]);
#pragma unroll
      for (int i = 8; i < 12; ++i) p[i] = EXP2(S1[i]);
      O = mfma32(Vf[2], pa, O);
#pragma unroll
      for (int i = 12; i < 16; ++i) p[i] = EXP2(S1[i]);
      f32x16 dq = mfma32(Aqk, eq_n1, zero16);
#pragma unroll
      for (int i = 0; i < 4; ++i) pb[i] = pk_bf16(p[8 + 2 * i], p[9 + 2 * i]);
#pragma unroll
      for (int i = 0; i < 4; ++i) p[i] = EXP2(S2[i]);
      O = mfma32(Vf[3], pb, O);
#pragma unroll
      for (int i = 4; i < 8; ++i) p[i] = EXP2(S2[i]);
      S3 = mfma32(Kf[3], Qs, Cb);
      u32x4 Qsn, Qa;
      {
#pragma unroll
        for (int i = 0; i < 4; ++i) pa[i] = pk_bf16(p[2 * i], p[2 * i + 1]);
#pragma unroll
        for (int i = 8; i < 10; ++i) p[i] = EXP2(S2[i]);
        unsigned q0 = pk_bf16(dq[0], dq[1]), q1 = pk_bf16(dq[2], dq[3]);
        unsigned q2 = pk_bf16(dq[4], dq[5]), q3 = pk_bf16(dq[6], dq[7]);
        Qsn[0] = q0 & m0; Qsn[1] = q1 & m0; Qsn[2] = q2 & m1; Qsn[3] = q3 & m1;
        Qa[0] = Qsn[0] | sg0; Qa[1] = Qsn[1] | sg0; Qa[2] = Qsn[2] | sg1; Qa[3] = Qsn[3] | sg1;
      }
      O = mfma32(Vf[4], pa, O);
#pragma unroll
      for (int i = 10; i < 14; ++i) p[i] = EXP2(S2[i]);
      f32x16 Cbn = mfma32(Akm, Qa, zero16);
#pragma unroll
      for (int i = 14; i < 16; ++i) p[i] = EXP2(S2[i]);
#pragma unroll
      for (int i = 0; i < 4; ++i) pb[i] = pk_bf16(p[8 + 2 * i], p[9 + 2 * i]);
#pragma unroll
      for (int i = 0; i < 2; ++i) p[i] = EXP2(S3[i]);
      O = mfma32(Vf[5], pb, O);
#pragma unroll
      for (int i = 2; i < 4; ++i) p[i] = EXP2(S3[i]);
      pa[0] = pk_bf16(p[0], p[1]) & halfmask;
      pa[1] = pk_bf16(p[2], p[3]) & halfmask;
      pa[2] = 0u; pa[3] = 0u;
      S0 = mfma32(Kf[0], Qsn, Cbn);
      O = mfma32(Vf[6], pa, O);
#pragma unroll
      for (int g = 0; g < 13; ++g) {
        __builtin_amdgcn_sched_group_barrier(0x400, 4, 0);
        __builtin_amdgcn_sched_group_barrier(0x8, 1, 0);
      }
      __builtin_amdgcn_sched_group_barrier(0x8, 1, 0);
      Qs = Qsn;
      Cb = Cbn;
      eq_prev = eq_cur;
      eq_cur = eq_n1;
      eq_n1 = eq_n2;
    }
    asm volatile("" :: "v"(touch));
    {
      const int tp = 6;
      EP1();
      EP2();
    }
#undef EP1
#undef EP2

    if (failmask) {
      for (int t = 0; t < 7; ++t) {
        if (!((failmask >> t) & 1u)) continue;
        u32x4 eq = LOAD_EQ(t);
        f32x16 dq = mfma32(Aqk, eq, zero16);
        u32x4 Qx;
        Qx[0] = pk_bf16(dq[0], dq[1]) & m0; Qx[1] = pk_bf16(dq[2], dq[3]) & m0;
        Qx[2] = pk_bf16(dq[4], dq[5]) & m1; Qx[3] = pk_bf16(dq[6], dq[7]) & m1;
        float mx = -INFINITY;
#pragma unroll
        for (int kt = 0; kt < 4; ++kt) {
          f32x16 S = mfma32(Kf[kt], Qx, zero16);
          if (kt < 3) {
#pragma unroll
            for (int i = 0; i < 16; ++i) mx = fmaxf(mx, S[i]);
          } else {
#pragma unroll
            for (int i = 0; i < 4; ++i) mx = fmaxf(mx, h ? -INFINITY : S[i]);
          }
        }
        mx = fmaxf(mx, __shfl_xor(mx, 32));
        f32x16 Cx;
#pragma unroll
        for (int i = 0; i < 16; ++i) Cx[i] = -mx;
        f32x16 Ox = zero16;
#pragma unroll
        for (int kt = 0; kt < 4; ++kt) {
          f32x16 S = mfma32(Kf[kt], Qx, Cx);
          if (kt < 3) {
            u32x4 p0, p1;
#pragma unroll
            for (int i = 0; i < 4; ++i) {
              p0[i] = pk_bf16(EXP2(S[2 * i]), EXP2(S[2 * i + 1]));
              p1[i] = pk_bf16(EXP2(S[8 + 2 * i]), EXP2(S[9 + 2 * i]));
            }
            Ox = mfma32(Vf[2 * kt], p0, Ox);
            Ox = mfma32(Vf[2 * kt + 1], p1, Ox);
          } else {
            u32x4 p0;
            p0[0] = pk_bf16(EXP2(S[0]), EXP2(S[1])) & halfmask;
            p0[1] = pk_bf16(EXP2(S[2]), EXP2(S[3])) & halfmask;
            p0[2] = 0u; p0[3] = 0u;
            Ox = mfma32(Vf[6], p0, Ox);
          }
        }
        const float rl = __builtin_amdgcn_rcpf(Ox[8]);
#pragma unroll
        for (int i = 0; i < 8; ++i) Ox[i] *= rl;
        Ox = mfma32(Ares, eq, Ox);
        float v0 = hd ? Ox[4] : Ox[0], v1 = hd ? Ox[5] : Ox[1];
        float v2 = hd ? Ox[6] : Ox[2], v3 = hd ? Ox[7] : Ox[3];
        u32x2 ov;
        ov[0] = pk_relu_bf16(pk_bf16(v0, v1));
        ov[1] = pk_relu_bf16(pk_bf16(v2, v3));
        __builtin_amdgcn_raw_buffer_store_b64(ov, rs_o, st_off + 512 * t, 0, 0);
      }
    }
#undef LOAD_EQ
  }
}

extern "C" void kernel_launch(void* const* d_in, const int* in_sizes, int n_in, void* d_out, int out_size, void* d_ws,
                              size_t ws_size, hipStream_t stream) {
  const unsigned short* e = (const unsigned short*)d_in[0];
  const unsigned short* wq = (const unsigned short*)d_in[1];
  const unsigned short* wk = (const unsigned short*)d_in[2];
  const unsigned short* wv = (const unsigned short*)d_in[3];
  const unsigned short* wres = (const unsigned short*)d_in[4];
  const int nbatch = in_sizes[0] / (F_ * D_);
  constexpr int NB = 2;
  const int nwaves = (nbatch + NB - 1) / NB;
  const int nblocks = (nwaves + 3) / 4;
  autoint_kernel<NB><<<nblocks, 256, 0, stream>>>(e, wq, wk, wv, wres, (unsigned short*)d_out, nbatch);
}
```
